```python
import torch

def _softmax(x, axis=-1):
    return torch.softmax(x, dim=axis)
def _gelu(x):
    return torch.nn.functional.gelu(x, approximate="tanh")
def _silu(x):
    return torch.nn.functional.silu(x)
def _sigmoid(x):
    return torch.sigmoid(x)
def _layernorm(x, axis=-1, eps=1e-5):
    m = x.mean(dim=axis, keepdim=True)
    v = x.var(dim=axis, keepdim=True, unbiased=False)
    return (x - m) / torch.sqrt(v + eps)
def _scatter_add(tgt, idx, src, axis=0):
    return tgt.clone().index_add_(axis, idx, src)
def _segment_sum(src, seg, n):
    out = torch.zeros((n,) + src.shape[1:], dtype=src.dtype)
    return out.index_add_(0, seg, src)
def _pad(x, pw):
    flat = tuple(v for pair in reversed(pw) for v in pair)
    return torch.nn.functional.pad(x, flat)

kernel_name = 'standalone__consistency_rf_step__diffusion'

def setup_inputs():

    _w = (lambda a: (lambda t: t.to(torch.bfloat16) if t.is_floating_point() else t)(torch.as_tensor(a)))
    B, C, HW = 2, 320, 128
    return {
        "x": _w((lambda *s: torch.randn(*s, dtype=torch.bfloat16) * 0.05)(B, C, HW, HW)),
        "v": _w((lambda *s: torch.randn(*s, dtype=torch.bfloat16) * 0.05)(B, C, HW, HW)),
        "sigma": _w(torch.rand(B).to(float)),
    }

def reference(x, v, sigma):
    s = sigma.reshape(-1, 1, 1, 1)
    return x - s * v
```

```cpp
#include <hip/hip_runtime.h>
#include <hip/hip_bf16.h>
#include <stdint.h>

typedef __attribute__((ext_vector_type(4))) unsigned int u32x4;
typedef __attribute__((ext_vector_type(2))) float f32x2;
typedef __attribute__((ext_vector_type(2))) __bf16 bf16x2;

__device__ __forceinline__ unsigned int step2(unsigned int xp, unsigned int vp, float s) {
    float x0 = __builtin_bit_cast(float, xp << 16);
    float x1 = __builtin_bit_cast(float, xp & 0xffff0000u);
    float v0 = __builtin_bit_cast(float, vp << 16);
    float v1 = __builtin_bit_cast(float, vp & 0xffff0000u);
    f32x2 p = {s * v0, s * v1};
    bf16x2 pb = __builtin_convertvector(p, bf16x2);
    unsigned int pu = __builtin_bit_cast(unsigned int, pb);
    float t0 = __builtin_bit_cast(float, pu << 16);
    float t1 = __builtin_bit_cast(float, pu & 0xffff0000u);
    f32x2 r = {x0 - t0, x1 - t1};
    bf16x2 rb = __builtin_convertvector(r, bf16x2);
    return __builtin_bit_cast(unsigned int, rb);
}

__global__ __launch_bounds__(256) void rf_step_vec(const u32x4* __restrict__ x, const u32x4* __restrict__ v,
                                                   const unsigned short* __restrict__ sigma,
                                                   u32x4* __restrict__ out, int nvec, int vec_per_batch) {
    int i = blockIdx.x * 256 + threadIdx.x;
    if (i >= nvec) return;
    int b = i / vec_per_batch;
    float s = __builtin_bit_cast(float, (unsigned int)sigma[b] << 16);
    u32x4 xv = x[i];
    u32x4 vv = v[i];
    u32x4 o;
    o.x = step2(xv.x, vv.x, s);
    o.y = step2(xv.y, vv.y, s);
    o.z = step2(xv.z, vv.z, s);
    o.w = step2(xv.w, vv.w, s);
    out[i] = o;
}

extern "C" void kernel_launch(void* const* d_in, const int* in_sizes, int n_in,
                              void* d_out, int out_size, void* d_ws, size_t ws_size,
                              hipStream_t stream) {
    int n = in_sizes[0];
    int B = in_sizes[2];
    int per_batch = n / B;
    int nvec = n / 8;
    rf_step_vec<<<(nvec + 255) / 256, 256, 0, stream>>>((const u32x4*)d_in[0], (const u32x4*)d_in[1],
                                                         (const unsigned short*)d_in[2], (u32x4*)d_out,
                                                         nvec, per_batch / 8);
}
```

```cpp
#include <hip/hip_runtime.h>
#include <stdint.h>

typedef __attribute__((ext_vector_type(4))) unsigned int u32x4;
typedef __attribute__((ext_vector_type(2))) float f32x2;
typedef __attribute__((ext_vector_type(2))) __bf16 bf16x2;

constexpr int BLOCK = 256;
constexpr int VPT = 4;
constexpr int LOAD_AUX = 2;
constexpr int STORE_AUX = 17;

__device__ __forceinline__ float bf_lo(unsigned p) { return __builtin_bit_cast(float, p << 16); }
__device__ __forceinline__ float bf_hi(unsigned p) { return __builtin_bit_cast(float, p & 0xffff0000u); }
__device__ __forceinline__ unsigned pack_bf(float a, float b) {
    f32x2 f = {a, b};
    return __builtin_bit_cast(unsigned, __builtin_convertvector(f, bf16x2));
}
__device__ __forceinline__ unsigned step2(unsigned xp, unsigned vp, float s) {
    const unsigned t = pack_bf(s * bf_lo(vp), s * bf_hi(vp));
    return pack_bf(bf_lo(xp) - bf_lo(t), bf_hi(xp) - bf_hi(t));
}

__global__ __launch_bounds__(BLOCK) __attribute__((aligned(2048))) void rf_step_vec(
    const char* __restrict__ x, const char* __restrict__ v, const unsigned short* __restrict__ sigma,
    char* __restrict__ out, unsigned batch_bytes) {
    const unsigned b = blockIdx.y;
    const size_t boff = (size_t)b * batch_bytes;
    const __amdgpu_buffer_rsrc_t rx = __builtin_amdgcn_make_buffer_rsrc((void*)(x + boff), 0, batch_bytes, 0x00020000);
    const __amdgpu_buffer_rsrc_t rv = __builtin_amdgcn_make_buffer_rsrc((void*)(v + boff), 0, batch_bytes, 0x00020000);
    const __amdgpu_buffer_rsrc_t ro = __builtin_amdgcn_make_buffer_rsrc((void*)(out + boff), 0, batch_bytes, 0x00020000);
    const unsigned off = blockIdx.x * (BLOCK * VPT * 16u) + threadIdx.x * 16u;

    const uintptr_t sa = (uintptr_t)(sigma + b);
    const unsigned sw = *(const __attribute__((address_space(4))) unsigned*)(sa & ~(uintptr_t)3);

    u32x4 xv[VPT], vv[VPT];
#pragma unroll
    for (int k = 0; k < VPT; ++k) {
        xv[k] = __builtin_amdgcn_raw_buffer_load_b128(rx, off + k * (BLOCK * 16u), 0, LOAD_AUX);
        vv[k] = __builtin_amdgcn_raw_buffer_load_b128(rv, off + k * (BLOCK * 16u), 0, LOAD_AUX);
        __builtin_amdgcn_sched_barrier(0);
    }
    const float s = (sa & 2) ? bf_hi(sw) : bf_lo(sw);
#pragma unroll
    for (int k = 0; k < VPT; ++k) {
        u32x4 o;
        o.x = step2(xv[k].x, vv[k].x, s);
        o.y = step2(xv[k].y, vv[k].y, s);
        o.z = step2(xv[k].z, vv[k].z, s);
        o.w = step2(xv[k].w, vv[k].w, s);
        __builtin_amdgcn_raw_buffer_store_b128(o, ro, off + k * (BLOCK * 16u), 0, STORE_AUX);
        __builtin_amdgcn_sched_barrier(0);
    }
}

__global__ __launch_bounds__(256) void rf_step_any(const unsigned short* __restrict__ x,
                                                   const unsigned short* __restrict__ v,
                                                   const unsigned short* __restrict__ sigma,
                                                   unsigned short* __restrict__ out, int n, int per_batch,
                                                   int stride) {
    for (long i = (long)blockIdx.x * 256 + threadIdx.x; i < n; i += stride) {
        const float s = bf_lo(sigma[(int)i / per_batch]);
        out[i] = (unsigned short)step2(x[i], v[i], s);
    }
}

extern "C" void kernel_launch(void* const* d_in, const int* in_sizes, int n_in, void* d_out, int out_size,
                              void* d_ws, size_t ws_size, hipStream_t stream) {
    const int n = in_sizes[0];
    const int nb = in_sizes[2];
    if (n <= 0 || nb <= 0 || n < nb) return;
    const int per_batch = n / nb;
    const uintptr_t al = (uintptr_t)d_in[0] | (uintptr_t)d_in[1] | (uintptr_t)d_out;
    constexpr unsigned chunk = BLOCK * VPT * 16u;
    if ((al & 15) == 0 && per_batch % 8 == 0 && nb <= 65535 && (size_t)per_batch * 2 <= 0x7fff0000u) {
        const unsigned batch_bytes = (unsigned)per_batch * 2u;
        dim3 grid((batch_bytes + chunk - 1) / chunk, nb);
        rf_step_vec<<<grid, BLOCK, 0, stream>>>((const char*)d_in[0], (const char*)d_in[1],
                                                (const unsigned short*)d_in[2], (char*)d_out, batch_bytes);
    } else {
        const int m = per_batch * nb;
        int blocks = (m + 255) / 256;
        if (blocks > 4096) blocks = 4096;
        rf_step_any<<<blocks, 256, 0, stream>>>((const unsigned short*)d_in[0], (const unsigned short*)d_in[1],
                                                (const unsigned short*)d_in[2], (unsigned short*)d_out, m, per_batch,
                                                blocks * 256);
    }
}
```
